```python
import torch

def _softmax(x, axis=-1):
    return torch.softmax(x, dim=axis)

def _gelu(x):
    return torch.nn.functional.gelu(x, approximate='tanh')

def _silu(x):
    return torch.nn.functional.silu(x)

def _sigmoid(x):
    return torch.sigmoid(x)

def _layernorm(x, axis=-1, eps=1e-05):
    m = x.mean(dim=axis, keepdim=True)
    v = x.var(dim=axis, keepdim=True, unbiased=False)
    return (x - m) / torch.sqrt(v + eps)

def _scatter_add(tgt, idx, src, axis=0):
    return tgt.clone().index_add_(axis, idx, src)

def _segment_sum(src, seg, n):
    out = torch.zeros((n,) + src.shape[1:], dtype=src.dtype)
    return out.index_add_(0, seg, src)

def _pad(x, pw):
    flat = tuple((v for pair in reversed(pw) for v in pair))
    return torch.nn.functional.pad(x, flat)
kernel_name = 'sequential_preLN__memory_aug_recurrent_mem_tr__dense_activation_silu__prompt'

def setup_inputs():

    _w = lambda a: (lambda t: t.to(torch.bfloat16) if t.is_floating_point() else t)(torch.as_tensor(a))

    def _A_setup():
        B, S, Dm = (1, 2048, 1024)
        H, Dh = (32, 128)
        M, seg = (8, 32)
        seg = min(seg, S)
        S = S // seg * seg
        return {'x': _w((lambda *s: torch.randn(*s, dtype=torch.bfloat16) * 0.05)(B, S, Dm)), 'mem0': _w((lambda *s: torch.randn(*s, dtype=torch.bfloat16) * 0.2)(B, M, Dm)), 'Wq': _w((lambda *s: torch.randn(*s, dtype=torch.bfloat16) * 1.6)(Dm, H, Dh) * Dm ** (-0.5)), 'Wk': _w((lambda *s: torch.randn(*s, dtype=torch.bfloat16) * 1.6)(Dm, H, Dh) * Dm ** (-0.5)), 'Wv': _w((lambda *s: torch.randn(*s, dtype=torch.bfloat16) * 0.2)(Dm, H, Dh) * Dm ** (-0.5)), 'Wo': _w((lambda *s: torch.randn(*s, dtype=torch.bfloat16) * 0.2)(H, Dh, Dm) * (H * Dh) ** (-0.5)), 'seg_len': seg}
    _A_in = _A_setup()

    def _B_setup():
        B, S, D, F = (1, 2048, 1024, 3584)
        return {'x': _w((lambda *s: torch.randn(*s, dtype=torch.bfloat16) * 0.05)(B, S, D)), 'w1': _w((lambda *s: torch.randn(*s, dtype=torch.bfloat16) * 0.8)(D, F) * D ** (-0.5)), 'w2': _w((lambda *s: torch.randn(*s, dtype=torch.bfloat16) * 0.1)(F, D) * F ** (-0.5))}
    _B_in = _B_setup()
    return {'A_x': _A_in['x'], 'A_mem0': _A_in['mem0'], 'A_Wq': _A_in['Wq'], 'A_Wk': _A_in['Wk'], 'A_Wv': _A_in['Wv'], 'A_Wo': _A_in['Wo'], 'A_seg_len': _A_in['seg_len'], 'B_x': _B_in['x'], 'B_w1': _B_in['w1'], 'B_w2': _B_in['w2']}

def reference(A_x, A_mem0, A_Wq, A_Wk, A_Wv, A_Wo, A_seg_len, B_x, B_w1, B_w2):

    def _A_ref(x, mem0, Wq, Wk, Wv, Wo, seg_len):
        B, S, Dm = x.shape
        H, Dh = (Wq.shape[1], Wq.shape[2])
        M = mem0.shape[1]
        scale = Dh ** (-0.5)
        n_seg = S // seg_len
        L = M + seg_len
        idx = torch.arange(L)
        mask = (idx[:, None] < M) | (idx[:, None] >= idx[None, :])
        mem = mem0
        outs = []
        for s in range(n_seg):
            xs = x[:, s * seg_len:(s + 1) * seg_len, :].clone()
            h = torch.concatenate([mem, xs], axis=1)
            q = torch.einsum('btd,dhe->bhte', h, Wq)
            k = torch.einsum('btd,dhe->bhte', h, Wk)
            v = torch.einsum('btd,dhe->bhte', h, Wv)
            scores = torch.einsum('bhqe,bhke->bhqk', q, k) * scale
            scores = torch.where(mask[None, None], scores, -1000000000.0)
            p = _softmax(scores, axis=-1)
            o = torch.einsum('bhqk,bhke->bhqe', p, v)
            o = torch.einsum('bhte,hed->btd', o, Wo)
            mem = o[:, :M, :].clone()
            outs.append(o[:, M:, :])
        return torch.concatenate(outs, axis=1)

    def _B_ref(x, w1, w2):
        h = torch.einsum('bsd,df->bsf', x, w1)
        h = _silu(h)
        out = torch.einsum('bsf,fd->bsd', h, w2)
        return out
    x = A_x
    h = x + _A_ref(_layernorm(x, axis=-1), A_mem0, A_Wq, A_Wk, A_Wv, A_Wo, A_seg_len)
    h2 = h + _B_ref(_layernorm(h, axis=-1), B_w1, B_w2)
    return h2
```

```cpp
#include <hip/hip_runtime.h>
#include <hip/hip_bf16.h>
typedef unsigned short u16;
__device__ __forceinline__ float b2f(u16 v) { return __uint_as_float(((unsigned)v) << 16); }
__device__ __forceinline__ u16 f2b(float f) { __hip_bfloat16 h = __float2bfloat16(f); return *(u16*)&h; }
namespace nv {
constexpr int S = 2048, D = 1024, H = 32, E = 128, M = 8, SEG = 32, F = 3584, NSEG = 64, HE = H * E;
__global__ void ln_k(const float* in, float* out) {
  int r = blockIdx.x, t = threadIdx.x; __shared__ float red[256];
  float s = 0; for (int i = t; i < D; i += 256) s += in[r * D + i];
  red[t] = s; __syncthreads(); for (int o = 128; o; o >>= 1) { if (t < o) red[t] += red[t + o]; __syncthreads(); }
  float mean = red[0] / D; __syncthreads();
  float q = 0; for (int i = t; i < D; i += 256) { float d = in[r * D + i] - mean; q += d * d; }
  red[t] = q; __syncthreads(); for (int o = 128; o; o >>= 1) { if (t < o) red[t] += red[t + o]; __syncthreads(); }
  float rstd = 1.0f / sqrtf(red[0] / D + 1e-5f);
  for (int i = t; i < D; i += 256) out[r * D + i] = b2f(f2b((in[r * D + i] - mean) * rstd));
}
__global__ void cvt_k(const u16* in, float* out, int n) { int i = blockIdx.x * 256 + threadIdx.x; if (i < n) out[i] = b2f(in[i]); }
__global__ void gemm_k(const float* A, int lda, const u16* W, int ldw, float* C, int ldc, int Mr, int N, int K, int act, const float* res) {
  __shared__ float As[16][65];
  int n = blockIdx.x * 64 + (threadIdx.x & 63), m0 = blockIdx.y * 16, ty = threadIdx.x >> 6;
  float acc[4] = {0, 0, 0, 0};
  for (int k0 = 0; k0 < K; k0 += 64) {
    for (int i = threadIdx.x; i < 16 * 64; i += 256) { int r = i >> 6, c = i & 63; As[r][c] = (m0 + r < Mr) ? A[(size_t)(m0 + r) * lda + k0 + c] : 0.f; }
    __syncthreads();
    for (int k = 0; k < 64; ++k) { float w = b2f(W[(size_t)(k0 + k) * ldw + n]);
      for (int j = 0; j < 4; ++j) acc[j] += As[ty * 4 + j][k] * w; }
    __syncthreads();
  }
  for (int j = 0; j < 4; ++j) { int m = m0 + ty * 4 + j; if (m < Mr) { float v = b2f(f2b(acc[j])); if (act == 1) v = b2f(f2b(v / (1.0f + __expf(-v)))); if (res) v = b2f(f2b(v + res[(size_t)m * ldc + n])); C[(size_t)m * ldc + n] = v; } }
}
__global__ void attn_k(const float* qm, const float* km, const float* vm, const float* qx, const float* kx, const float* vx, float* om, float* ox) {
  int h = blockIdx.x, r = blockIdx.y, t = threadIdx.x;
  __shared__ float p[40];
  const float* q = r < M ? qm + r * HE : qx + (r - M) * HE;
  if (t < 40) { const float* k = t < M ? km + t * HE : kx + (t - M) * HE; float s = 0; for (int e = 0; e < E; ++e) s += q[h * E + e] * k[h * E + e];
    s = b2f(f2b(b2f(f2b(s)) * 0.08838834764831845f)); if (!(r < M || r >= t)) s = -1e9f; p[t] = s; }
  __syncthreads();
  float mx = -1e30f; for (int j = 0; j < 40; ++j) mx = fmaxf(mx, p[j]);
  float sum = 0; for (int j = 0; j < 40; ++j) sum += __expf(p[j] - mx);
  float o = 0; for (int j = 0; j < 40; ++j) { const float* v = j < M ? vm + j * HE : vx + (j - M) * HE; o += b2f(f2b(__expf(p[j] - mx) / sum)) * v[h * E + t]; }
  o = b2f(f2b(o));
  if (r < M) om[r * HE + h * E + t] = o; else ox[(r - M) * HE + h * E + t] = o;
}
__global__ void out_k(const float* in, u16* out, int n) { int i = blockIdx.x * 256 + threadIdx.x; if (i < n) out[i] = f2b(in[i]); }
struct Bufs { float *x, *xn, *q, *k, *v, *o, *mem, *qm, *km, *vm, *om, *h, *hn, *hid, *out; };
inline Bufs carve(void* ws) { float* p = (float*)ws; Bufs b; auto take = [&](size_t n) { float* r = p; p += n; return r; };
  b.x = take(S * D); b.xn = take(S * D); b.q = take((size_t)S * HE); b.k = take((size_t)S * HE); b.v = take((size_t)S * HE); b.o = take((size_t)S * HE);
  b.mem = take((NSEG + 1) * M * D); b.qm = take(NSEG * M * HE); b.km = take(NSEG * M * HE); b.vm = take(NSEG * M * HE); b.om = take(NSEG * M * HE);
  b.h = take(S * D); b.hn = take(S * D); b.hid = take((size_t)S * F); b.out = take(S * D); return b; }
inline void run(void* const* d_in, void* d_out, void* d_ws, hipStream_t st) {
  const u16* x = (const u16*)d_in[0]; const u16* mem0 = (const u16*)d_in[1]; const u16* Wq = (const u16*)d_in[2]; const u16* Wk = (const u16*)d_in[3]; const u16* Wv = (const u16*)d_in[4]; const u16* Wo = (const u16*)d_in[5];
  const u16* w1 = (const u16*)d_in[8]; const u16* w2 = (const u16*)d_in[9];
  Bufs b = carve(d_ws);
  cvt_k<<<S * D / 256, 256, 0, st>>>(x, b.x, S * D); cvt_k<<<M * D / 256, 256, 0, st>>>(mem0, b.mem, M * D);
  ln_k<<<S, 256, 0, st>>>(b.x, b.xn);
  gemm_k<<<dim3(HE / 64, S / 16), 256, 0, st>>>(b.xn, D, Wq, HE, b.q, HE, S, HE, D, 0, nullptr);
  gemm_k<<<dim3(HE / 64, S / 16), 256, 0, st>>>(b.xn, D, Wk, HE, b.k, HE, S, HE, D, 0, nullptr);
  gemm_k<<<dim3(HE / 64, S / 16), 256, 0, st>>>(b.xn, D, Wv, HE, b.v, HE, S, HE, D, 0, nullptr);
  for (int s = 0; s < NSEG; ++s) {
    float* mem = b.mem + s * M * D; size_t mo = (size_t)s * M * HE, xo = (size_t)s * SEG * HE;
    gemm_k<<<dim3(HE / 64, 1), 256, 0, st>>>(mem, D, Wq, HE, b.qm + mo, HE, M, HE, D, 0, nullptr);
    gemm_k<<<dim3(HE / 64, 1), 256, 0, st>>>(mem, D, Wk, HE, b.km + mo, HE, M, HE, D, 0, nullptr);
    gemm_k<<<dim3(HE / 64, 1), 256, 0, st>>>(mem, D, Wv, HE, b.vm + mo, HE, M, HE, D, 0, nullptr);
    attn_k<<<dim3(H, 40), 128, 0, st>>>(b.qm + mo, b.km + mo, b.vm + mo, b.q + xo, b.k + xo, b.v + xo, b.om + mo, b.o + xo);
    gemm_k<<<dim3(D / 64, 1), 256, 0, st>>>(b.om + mo, HE, Wo, D, mem + M * D, D, M, D, HE, 0, nullptr);
  }
  gemm_k<<<dim3(D / 64, S / 16), 256, 0, st>>>(b.o, HE, Wo, D, b.h, D, S, D, HE, 0, b.x);
  ln_k<<<S, 256, 0, st>>>(b.h, b.hn);
  gemm_k<<<dim3(F / 64, S / 16), 256, 0, st>>>(b.hn, D, w1, F, b.hid, F, S, F, D, 1, nullptr);
  gemm_k<<<dim3(D / 64, S / 16), 256, 0, st>>>(b.hid, F, w2, D, b.out, D, S, D, F, 0, b.h);
  out_k<<<S * D / 256, 256, 0, st>>>(b.out, (u16*)d_out, S * D);
}
}
extern "C" void kernel_launch(void* const* d_in, const int* in_sizes, int n_in, void* d_out, int out_size, void* d_ws, size_t ws_size, hipStream_t stream) {
  nv::run(d_in, d_out, d_ws, stream);
}
```

```cpp
#include <hip/hip_runtime.h>
typedef unsigned short u16;
typedef short s16x4 __attribute__((ext_vector_type(4)));
typedef short s16x8 __attribute__((ext_vector_type(8)));
typedef short s16x16 __attribute__((ext_vector_type(16)));
typedef __bf16 bf16x8 __attribute__((ext_vector_type(8)));
typedef __bf16 bf16x16 __attribute__((ext_vector_type(16)));
typedef float f32x4 __attribute__((ext_vector_type(4)));
typedef float f32x2 __attribute__((ext_vector_type(2)));
typedef unsigned u32x4 __attribute__((ext_vector_type(4)));
typedef unsigned u32x2 __attribute__((ext_vector_type(2)));
#define LAS __attribute__((address_space(3)))
#define GAS __attribute__((address_space(1)))
#define DEV __device__ __forceinline__
typedef LAS char* ldsp;
typedef __amdgpu_buffer_rsrc_t rsrc_t;

DEV unsigned pk_bf16(float lo, float hi) { typedef __bf16 b2 __attribute__((ext_vector_type(2))); f32x2 v = {lo, hi}; b2 r = __builtin_convertvector(v, b2); return __builtin_bit_cast(unsigned, r); }
DEV float bf_lo(unsigned v) { return __uint_as_float(v << 16); }
DEV float bf_hi(unsigned v) { return __uint_as_float(v & 0xffff0000u); }
DEV bf16x8 lds_rd128(ldsp p) { return *(LAS bf16x8*)p; }
DEV s16x4 lds_tr(ldsp p) { return __builtin_amdgcn_ds_read_tr16_b64_v4i16((LAS s16x4*)p); }
#define WAIT_VM(n) asm volatile("s_waitcnt vmcnt(%0)" :: "n"(n) : "memory")
#define WAIT_LGKM0() __builtin_amdgcn_s_waitcnt(0xC07F)
#define BAR() do { asm volatile("" ::: "memory"); __builtin_amdgcn_s_barrier(); asm volatile("" ::: "memory"); } while (0)
#define LBAR() do { asm volatile("s_waitcnt lgkmcnt(0)" ::: "memory"); __builtin_amdgcn_s_barrier(); asm volatile("" ::: "memory"); } while (0)
#define SCHED() __builtin_amdgcn_sched_barrier(0)
#define RLX_AGENT __ATOMIC_RELAXED, __HIP_MEMORY_SCOPE_AGENT
DEV rsrc_t mk_rsrc(const void* p, unsigned bytes) { return __builtin_amdgcn_make_buffer_rsrc((void*)p, 0, bytes, 0x00020000); }
DEV void glds16(const char* g, ldsp l) { __builtin_amdgcn_global_load_lds((const GAS unsigned*)g, (LAS unsigned*)l, 16, 0, 0); }
DEV void glds16_sc1(const char* g, ldsp l) { __builtin_amdgcn_global_load_lds((const GAS unsigned*)g, (LAS unsigned*)l, 16, 0, 16); }
DEV void blds16_sc1(rsrc_t r, int voff, int soff, ldsp l) { __builtin_amdgcn_raw_ptr_buffer_load_lds(r, (LAS void*)l, 16, voff, soff, 0, 16); }
DEV float vmax2(float a, float b) { return __builtin_elementwise_maximum(a, b); }
DEV float vmax3(float a, float b, float c) { return vmax2(vmax2(a, b), c); }
DEV float xor16_max(float x) { auto r = __builtin_amdgcn_permlane16_swap(__float_as_uint(x), __float_as_uint(x), false, false); return vmax2(__uint_as_float(r[0]), __uint_as_float(r[1])); }
DEV float xor32_max(float x) { auto r = __builtin_amdgcn_permlane32_swap(__float_as_uint(x), __float_as_uint(x), false, false); return vmax2(__uint_as_float(r[0]), __uint_as_float(r[1])); }
DEV float xor16_add(float x) { auto r = __builtin_amdgcn_permlane16_swap(__float_as_uint(x), __float_as_uint(x), false, false); return __uint_as_float(r[0]) + __uint_as_float(r[1]); }
DEV float xor32_add(float x) { auto r = __builtin_amdgcn_permlane32_swap(__float_as_uint(x), __float_as_uint(x), false, false); return __uint_as_float(r[0]) + __uint_as_float(r[1]); }
DEV float fold2(float a, float b) { auto r = __builtin_amdgcn_permlane32_swap(__float_as_uint(a), __float_as_uint(b), false, false); return __uint_as_float(r[0]) + __uint_as_float(r[1]); }

DEV int lane_id() { int l; asm volatile("v_mbcnt_lo_u32_b32 %0, -1, 0\n\tv_mbcnt_hi_u32_b32 %0, -1, %0" : "=v"(l)); return l; }
DEV unsigned xcc_id() { return (unsigned)__builtin_amdgcn_s_getreg(20 | (3 << 11)) & 7u; }
constexpr int S_ = 2048, D_ = 1024, H_ = 32, HE_ = 4096, F_ = 3584, NSEG = 64;
constexpr unsigned SENT = 0xFFFFFFFFu;
constexpr size_t WS_CTRL = 0, CTRL_BYTES = 65536;
constexpr size_t WS_XN = 1u << 20;
constexpr size_t WS_XT = WS_XN + (4u << 20);
constexpr size_t WS_HT = WS_XT + (4u << 20);
constexpr size_t WS_STATS = WS_HT + (4u << 20);
constexpr size_t WS_CS1 = WS_STATS + (1u << 20);
constexpr size_t WS_XCH = WS_CS1 + (1u << 20);
constexpr int X_MEM = 0, X_HDR = (1 << 20) + 65536, X_TMO = (1 << 20) + 131072, X_O = 2 << 20, X_V = 6 << 20, X_LOGICAL = 10 << 20;
constexpr unsigned XCH_BYTES = 20u << 20;
__host__ __device__ constexpr int xmap(int x) { return ((x >> 13) << 14) | (x & 8191) | (((x >> 20) & 1) << 13); }
constexpr size_t WS_OT = WS_XCH + (28u << 20);
constexpr size_t WS_HID = WS_OT + (16u << 20);
constexpr size_t WS_QKV = WS_HID + (14u << 20);
constexpr size_t WS_DBG = WS_QKV + (48u << 20);
struct Params { const char *x, *mem0, *wq, *wk, *wv, *wo, *w1, *w2; char* out; char* ws; unsigned xa, xb, dl, rec; };
__host__ __device__ inline char* xch_base(const void* ws) { return (char*)(((size_t)ws + WS_XCH + (2u << 20) - 1) & ~(size_t)((2u << 20) - 1)); }
DEV unsigned nosent(unsigned d) { return __builtin_elementwise_min(d, 0xFFFFFFFEu); }
DEV u32x4 pack8(const f32x4& a, const f32x4& b) { return (u32x4){pk_bf16(a[0], a[1]), pk_bf16(a[2], a[3]), pk_bf16(b[0], b[1]), pk_bf16(b[2], b[3])}; }

__global__ void __launch_bounds__(512) k_prologue(Params p) {
  __shared__ float red[256];
  const int b = blockIdx.x, tid = threadIdx.x, wid = tid >> 6, lane = tid & 63;
  if (b < 128) {
    const char* xr = p.x + (size_t)(b * 16 + (lane & 15)) * 2048 + (lane >> 4) * 8;
    u32x4 v[4];
#pragma unroll
    for (int i = 0; i < 4; ++i) { u32x2 a = *(const u32x2*)(xr + (wid * 4 + i) * 64), c = *(const u32x2*)(xr + (wid * 4 + i) * 64 + 32); v[i] = (u32x4){a[0], a[1], c[0], c[1]}; }
    float s = 0.f;
#pragma unroll
    for (int i = 0; i < 4; ++i)
#pragma unroll
      for (int j = 0; j < 4; ++j) s += bf_lo(v[i][j]) + bf_hi(v[i][j]);
    s = xor32_add(xor16_add(s));
    if (lane < 16) red[wid * 16 + lane] = s;
    __syncthreads();
    float mean = 0.f;
#pragma unroll
    for (int w = 0; w < 8; ++w) mean += red[w * 16 + (lane & 15)];
    mean *= (1.f / 1024.f);
    float q = 0.f;
#pragma unroll
    for (int i = 0; i < 4; ++i)
#pragma unroll
      for (int j = 0; j < 4; ++j) { float a = bf_lo(v[i][j]) - mean, c = bf_hi(v[i][j]) - mean; q += a * a + c * c; }
    q = xor32_add(xor16_add(q));
    if (lane < 16) red[128 + wid * 16 + lane] = q;
    __syncthreads();
    float var = 0.f;
#pragma unroll
    for (int w = 0; w < 8; ++w) var += red[128 + w * 16 + (lane & 15)];
    float rstd = 1.0f / sqrtf(var * (1.f / 1024.f) + 1e-5f);
#pragma unroll
    for (int i = 0; i < 4; ++i) { u32x4 o;
#pragma unroll
      for (int j = 0; j < 4; ++j) o[j] = pk_bf16((bf_lo(v[i][j]) - mean) * rstd, (bf_hi(v[i][j]) - mean) * rstd);
      size_t off = (size_t)(b * 32 + wid * 4 + i) * 1024 + lane * 16;
      *(u32x4*)(p.ws + WS_XN + off) = o; *(u32x4*)(p.ws + WS_XT + off) = v[i]; }
  } else {
    const int nb = 128, i0 = b - 128;
    const u32x4 sv = {SENT, SENT, SENT, SENT};
    for (int o = 16384 + (i0 * 512 + tid) * 16; o < X_LOGICAL; o += nb * 512 * 16) if (o < X_TMO + 8192 || o >= X_O) *(u32x4*)(xch_base(p.ws) + xmap(o)) = sv;
    if (i0 < 1) {
#pragma unroll
      for (int i = 0; i < 2; ++i) { int c = i * 512 + tid, t = c >> 6, l = c & 63;
        u32x4 v = {0, 0, 0, 0};
        if (i0 == 0) { v = *(const u32x4*)(p.mem0 + ((l & 7) * 1024 + t * 64 + (l >> 4) * 16 + ((l >> 3) & 1) * 8) * 2);
#pragma unroll
          for (int j = 0; j < 4; ++j) v[j] = nosent(v[j]); }
        *(u32x4*)(xch_base(p.ws) + xmap(c * 16)) = v; }
    }
    if (i0 == 2) for (int o = tid * 16; o < (int)CTRL_BYTES; o += 512 * 16) if (o != 2048) *(u32x4*)(p.ws + WS_CTRL + o) = (u32x4){0, 0, 0, 0};
    if (i0 == 3 && wid == 0) {
      rsrc_t rx = mk_rsrc(xch_base(p.ws), XCH_BYTES); unsigned best[2] = {~0u, ~0u};
      for (int r = 0; r < 8; ++r) {
        const int o = (xmap(X_TMO + 16384) ^ ((r & 1) << 13)) + (r >> 1) * 256;
        WAIT_VM(0); unsigned long long t0, t1;
        asm volatile("s_memtime %0\n\ts_waitcnt lgkmcnt(0)" : "=s"(t0) :: "memory");
        __builtin_amdgcn_raw_buffer_store_b32(0, rx, lane ? 1 << 30 : 0, o, 16);
        WAIT_VM(0);
        asm volatile("s_memtime %0\n\ts_waitcnt lgkmcnt(0)" : "=s"(t1) :: "memory");
        const unsigned d = (unsigned)(t1 - t0); if (d < best[r & 1]) best[r & 1] = d; }
      if (lane == 0) *(u32x4*)(p.ws + WS_CTRL + 2048) = (u32x4){((xcc_id() >> 2) & 1) ^ (best[0] <= best[1] ? 0u : 1u), best[0], best[1], 0u};
    }
  }
}
constexpr int NRT = 13;
constexpr int L_MEMS = 0, L_SC = 0, L_MX = 4096, L_TOK = 16384, L_QKM = L_TOK + 24576, L_HDR = L_QKM + 4096, L_WL = 45312, L_WX = L_WL + 98304, L_QK_END = L_WX + 16384;
constexpr unsigned SPIN_CAP = 1u << 18;
#define STAMP(i)
#define STAMP_DECL
#define STAMP_FLUSH(role, s)

DEV bool give_up(unsigned& spins, rsrc_t rx) {
  asm volatile("" ::: "memory");
  if ((++spins & 1023u) != 0u) return false;
  if (spins > SPIN_CAP) { int z = 0; asm volatile("" : "+v"(z)); __builtin_amdgcn_raw_buffer_store_b32(z, rx, 0, xmap(X_TMO), 16); return true; }
  return (unsigned)__builtin_amdgcn_readfirstlane(__builtin_amdgcn_raw_buffer_load_b32(rx, 0, xmap(X_TMO), 16)) != SENT;
}
DEV bool give_up_uniform(unsigned& spins) { return ++spins > (SPIN_CAP >> 4); }
#define POLL_SLEEP 6
#define TOK_SLEEP 6
#define PRIO_FLIP 11
DEV unsigned umax3(unsigned a, unsigned b, unsigned c) { return __builtin_elementwise_max(__builtin_elementwise_max(a, b), c); }
DEV bool all_ne_sent(unsigned m) { return __builtin_amdgcn_ballot_w64(m == SENT) == 0ull; }
DEV int opaque(int v) { asm volatile("" : "+v"(v)); return v; }
DEV unsigned now32() { return (unsigned)__builtin_amdgcn_s_memrealtime(); }
DEV void wait_until(unsigned t) { for (;;) { int d = (int)(t - now32()); if (d <= 0) break; if (d > 12) __builtin_amdgcn_s_sleep(3); } }
template <int N, bool HDR> DEV bool gather_try(rsrc_t r, int voff, int soff, u32x4 (&v)[N], int hoff, u32x2& hd) {
#pragma unroll
  for (int i = 0; i < N; ++i) v[i] = __builtin_amdgcn_raw_buffer_load_b128(r, voff + (i & 3) * 1024, soff + (i >> 2) * 4096, 16);
  unsigned m = 0;
  if (HDR) { hd = __builtin_amdgcn_raw_buffer_load_b64(r, 0, hoff, 16); m = hd[0]; }
#pragma unroll
  for (int i = 0; i < N; ++i) m = umax3(m, v[i][0], v[i][2]);
  return all_ne_sent(m);
}
#define TL(role, k)
#define COUNT_FAIL(k)
#define FLUSH_FAIL()
template <int N, bool HDR> DEV void gather(const Params& p, rsrc_t r, int voff, int soff, u32x4 (&v)[N], int hoff, u32x2& hd, int site, unsigned& nfail) {
  if (__builtin_expect(gather_try<N, HDR>(r, voff, soff, v, hoff, hd), 1)) return;
  COUNT_FAIL(site);
  unsigned spins = 0;
  while (!gather_try<N, HDR>(r, voff, soff, v, hoff, hd)) if (give_up(spins, r)) break;
}
DEV void lds_add(ldsp a, float v) { __hip_atomic_fetch_add((LAS float*)a, v, __ATOMIC_RELAXED, __HIP_MEMORY_SCOPE_WORKGROUP); }
DEV f32x4 smf(const bf16x8& a, const bf16x16& b, const f32x4& c, int idx) { return __builtin_amdgcn_smfmac_f32_16x16x64_bf16(a, b, c, idx, 0, 0); }
DEV f32x4 mfma16(u32x2 a, u32x2 b, const f32x4& c) { return __builtin_amdgcn_mfma_f32_16x16x16bf16_1k(__builtin_bit_cast(s16x4, a), __builtin_bit_cast(s16x4, b), c, 0, 0, 0); }
DEV bf16x16 tr_frag(ldsp a, int ustep_lo, int ustep_hi) {
  s16x4 u0 = lds_tr(a), u1 = lds_tr(a + ustep_lo), u2 = lds_tr(a + ustep_hi), u3 = lds_tr(a + ustep_hi + ustep_lo);
  s16x8 lo = __builtin_shufflevector(u0, u1, 0, 1, 2, 3, 4, 5, 6, 7), hi = __builtin_shufflevector(u2, u3, 0, 1, 2, 3, 4, 5, 6, 7);
  return __builtin_bit_cast(bf16x16, __builtin_shufflevector(lo, hi, 0, 1, 2, 3, 4, 5, 6, 7, 8, 9, 10, 11, 12, 13, 14, 15)); }
DEV bf16x16 lds_frag(ldsp a) { bf16x8 lo = lds_rd128(a), hi = lds_rd128(a + 1024); return __builtin_shufflevector(lo, hi, 0, 1, 2, 3, 4, 5, 6, 7, 8, 9, 10, 11, 12, 13, 14, 15); }

constexpr int C_TICKET = 0;
constexpr int C_IOD = 2048;
constexpr int PW = 128;
constexpr int C_PROG_P = 16384, C_PROG_QK = C_PROG_P + 64 * PW, C_PROG_O = C_PROG_QK + 32 * PW, C_PROG_F1 = C_PROG_O + 64 * PW;
DEV void publish_progress(rsrc_t rc, int off, int val) { __builtin_amdgcn_raw_buffer_store_b32(val, rc, lane_id() ? 1 << 30 : 0, off, 16); }
DEV int wait_progress(rsrc_t rc, rsrc_t rx, int off, int n, int need) {
  const int l = lane_id(), vo = (l < n ? l : 0) * PW;
  for (unsigned spins = 0;;) {
    int v = __builtin_amdgcn_raw_buffer_load_b32(rc, vo, off, 16);
    if (__builtin_amdgcn_ballot_w64(v < need) == 0ull) {
      int m = need; while (m < NSEG && __builtin_amdgcn_ballot_w64(v < m + 1) == 0ull) ++m;
      return m; }
    if (give_up(spins, rx)) return NSEG;
    __builtin_amdgcn_s_sleep(POLL_SLEEP);
  }
}
DEV void role_qk(const Params& p, int wid, ldsp lds, int h) {
  const int lane = lane_id();
  const int fr = lane & 15, g = lane >> 4;
  TL(h, 64);
  bf16x16 Wq[NRT], Wk[NRT - 1];
  {
    unsigned so[2];
#pragma unroll
    for (int i = 0; i < 2; ++i) { int row = (wid * 2 + i) * 4 + g, f = (row & 1) | (((row >> 2) & 3) << 1); so[i] = (unsigned)((row * HE_ + h * 128 + (fr ^ (f << 1)) * 8) * 2); }
    auto stage = [&](int t) { ldsp b = lds + L_WL + (t % 3) * 32768 + wid * 2048; size_t go = (size_t)t * 64 * HE_ * 2;
      glds16(p.wq + go + so[0], b); glds16(p.wq + go + so[1], b + 1024); glds16(p.wk + go + so[0], b + 16384); glds16(p.wk + go + so[1], b + 16384 + 1024); };
    const int q = fr >> 2, f = (q & 1) | ((g & 1) << 1) | ((q >> 1) << 2);
    const int rb = (16 * (g >> 1) + 4 * (g & 1) + (q & 1) + 8 * (q >> 1)) * 256 + ((wid ^ f) * 32) + (lane & 3) * 8;
    stage(0); stage(1); stage(2);
#pragma unroll
    for (int t = 0; t < 16; ++t) {
      if (t < 14) WAIT_VM(8); else if (t == 14) WAIT_VM(4); else WAIT_VM(0);
      BAR();
      ldsp b = lds + L_WL + (t % 3) * 32768 + rb;
      bf16x16 a = tr_frag(b, 512, 8192), c = tr_frag(b + 16384, 512, 8192);
      if (t < NRT) Wq[t < NRT ? t : 0] = a;
      if (t < NRT - 1) Wk[t < NRT - 1 ? t : 0] = c;
      if (t == NRT - 1) { ldsp d = lds + L_WX + wid * 2048 + lane * 16;
        *(LAS bf16x8*)d = __builtin_shufflevector(c, c, 0, 1, 2, 3, 4, 5, 6, 7); *(LAS bf16x8*)(d + 1024) = __builtin_shufflevector(c, c, 8, 9, 10, 11, 12, 13, 14, 15); }
      LBAR();
      if (t + 3 < 16) stage(t + 3);
      if (t >= NRT) {
        ldsp d = lds + L_WL + (t % 3) * 32768 + wid * 4096 + lane * 16;
        *(LAS bf16x8*)d = __builtin_shufflevector(a, a, 0, 1, 2, 3, 4, 5, 6, 7); *(LAS bf16x8*)(d + 1024) = __builtin_shufflevector(a, a, 8, 9, 10, 11, 12, 13, 14, 15);
        *(LAS bf16x8*)(d + 2048) = __builtin_shufflevector(c, c, 0, 1, 2, 3, 4, 5, 6, 7); *(LAS bf16x8*)(d + 3072) = __builtin_shufflevector(c, c, 8, 9, 10, 11, 12, 13, 14, 15); }
    }
  }
  rsrc_t rx = mk_rsrc(xch_base(p.ws), XCH_BYTES), rot = mk_rsrc(p.ws + WS_OT, 16u << 20);
  const int l16 = lane * 16;
  rsrc_t rqkv = mk_rsrc(p.ws + WS_QKV, 48u << 20);
  auto tok_dma = [&](int s) {
#pragma unroll
    for (int i = 0; i < 3; ++i) blds16_sc1(rqkv, l16, (s * 32 + h) * 24576 + wid * 3072 + i * 1024, lds + L_TOK + wid * 3072 + i * 1024); };
  TL(h, 65);
  rsrc_t rc = mk_rsrc(p.ws + WS_CTRL, CTRL_BYTES);
  int avail = wait_progress(rc, rx, C_PROG_P + h * 2 * PW, 2, 1);
  tok_dma(0);
  constexpr float SC = 0.12751743f;
  const f32x4 z4 = {0.f, 0.f, 0.f, 0.f};
  STAMP_DECL;
  unsigned t_mem = 0, nfail = 0;
  for (int s = 0; s < NSEG; ++s) {
    STAMP(0);
    const int idx = ((opaque(l16) >> 7) & 1) * 0xAAAA + 0x4444;
    const int msoff = xmap(s * 16384 + wid * 2048);
    if (s > 0) wait_until(t_mem);
    STAMP(10);
    f32x4 aq, ak;
    int qkw, qw1, qw2, qw3;
    for (unsigned spins = 0;;) {
      blds16_sc1(rx, l16, msoff, lds + L_MEMS + wid * 2048); blds16_sc1(rx, l16, msoff + 1024, lds + L_MEMS + wid * 2048 + 1024);
      if (wid == 7) __builtin_amdgcn_raw_ptr_buffer_load_lds(rx, (LAS void*)(lds + L_HDR), 4, opaque(l16) >> 2, xmap(X_HDR + s * 256), 0, 16);
      bf16x16 B0, B1;
      { ldsp w = lds + L_WL + (13 % 3) * 32768 + wid * 4096 + opaque(l16); B0 = lds_frag(w); B1 = lds_frag(w + 2048); }
      aq = z4; ak = z4;
      unsigned chk = 0;
      if (wid >= 4) __builtin_amdgcn_s_setprio(1);
      WAIT_VM(0);
      STAMP(1);
      BAR();
      STAMP(2);
      constexpr int ORD[16] = {13, 0, 1, 2, 3, 14, 4, 5, 6, 7, 15, 8, 9, 10, 11, 12};
      bf16x8 a[4], h0, h1; ldsp wb;
#pragma unroll
      for (int i = 0; i < 3; ++i) a[i] = lds_rd128(lds + L_MEMS + ORD[i] * 1024 + l16);
      SCHED();
#pragma unroll
      for (int i = 0; i < 16; ++i) {
        const int t = ORD[i];
        aq = smf(a[i & 3], t < NRT ? Wq[t < NRT ? t : 0] : B0, aq, idx);
        SCHED();
        if (i + 3 < 16) a[(i + 3) & 3] = lds_rd128(lds + L_MEMS + ORD[i + 3 < 16 ? i + 3 : 0] * 1024 + l16);
        { u32x4 u = __builtin_bit_cast(u32x4, a[i & 3]); chk = umax3(chk, u[0], u[2]); }
        SCHED();
        ak = smf(a[i & 3], t < NRT - 1 ? Wk[t < NRT - 1 ? t : 0] : B1, ak, idx);
        SCHED();
        if (i == 0 || i == 5) { wb = lds + L_WL + ((t + 1) % 3) * 32768 + wid * 4096 + opaque(l16); h0 = lds_rd128(wb); }
        if (i == 1 || i == 6) { h1 = lds_rd128(wb + 1024); B0 = __builtin_shufflevector(h0, h1, 0, 1, 2, 3, 4, 5, 6, 7, 8, 9, 10, 11, 12, 13, 14, 15); }
        if (i == 2 || i == 7) h0 = lds_rd128(wb + 2048);
        if (i == 3 || i == 8) { h1 = lds_rd128(wb + 3072); B1 = __builtin_shufflevector(h0, h1, 0, 1, 2, 3, 4, 5, 6, 7, 8, 9, 10, 11, 12, 13, 14, 15); }
        if (i == 10) { wb = lds + L_WX + wid * 2048 + opaque(l16); h0 = lds_rd128(wb); }
        if (i == 11) { h1 = lds_rd128(wb + 1024); B1 = __builtin_shufflevector(h0, h1, 0, 1, 2, 3, 4, 5, 6, 7, 8, 9, 10, 11, 12, 13, 14, 15); }
        if (i == PRIO_FLIP) __builtin_amdgcn_s_setprio(0);
        if (i == 12) { const int ln = opaque(l16) >> 4; qkw = L_QKM + (wid >> 1) * 1024 + (wid & 1) * 8 + ((ln >> 4) * 4 + 17 * ((ln & 15) >> 2)) * 16 + (ln & 3) * 2; qw1 = qkw ^ 16; qw2 = qkw ^ 32; qw3 = qkw ^ 48; asm volatile("" : "+v"(qkw), "+v"(qw1), "+v"(qw2), "+v"(qw3)); }
        SCHED();
      }
      if (__builtin_expect(all_ne_sent(chk), 1)) break;
      if (spins == 0) COUNT_FAIL(0);
      { u32x4 m[2]; u32x2 hx; unsigned sp = 0;
        while (!gather_try<2, false>(rx, l16, msoff, m, 0, hx)) if (give_up(sp, rx)) break; }
      LBAR();
      if (++spins > 3) break;
    }
    STAMP(3);
    const int o = opaque(l16), vvo = (o >> 1) & 255, osw = o ^ ((o >> 4) & 48);
    const int vsoff = xmap(X_V + s * 65536 + h * 2048);
    auto fold_store = [&]() { float f[4];
#pragma unroll
      for (int i = 0; i < 4; ++i) f[i] = fold2(aq[i], ak[i]);
      unsigned u0 = pk_bf16(f[0], f[1]), u1 = pk_bf16(f[2], f[3]);
      *(LAS u16*)(lds + qkw) = (u16)u0; *(LAS u16*)(lds + qw1) = (u16)(u0 >> 16); *(LAS u16*)(lds + qw2) = (u16)u1; *(LAS u16*)(lds + qw3) = (u16)(u1 >> 16); SCHED(); };
    fold_store();
    if (wid < 4) {
      u32x2 vm0, vm1;
      auto vload = [&]() { vm0 = __builtin_amdgcn_raw_buffer_load_b64(rx, vvo, vsoff + wid * 512, 16); vm1 = __builtin_amdgcn_raw_buffer_load_b64(rx, vvo + 256, vsoff + wid * 512, 16); };
      bf16x8 ka[4], qb[4];
#pragma unroll
      for (int k = 0; k < 4; ++k) ka[k] = lds_rd128(lds + L_TOK + 8192 + (wid >> 1) * 4096 + k * 1024 + o);
      vload();
      __builtin_amdgcn_s_setprio(2);
      SCHED();
      asm volatile("s_waitcnt lgkmcnt(4)" ::: "memory");
      BAR(); STAMP(4);
#pragma unroll
      for (int k = 0; k < 4; ++k) qb[k] = lds_rd128(lds + L_QKM + k * 1024 + osw);
      if (wid == 0) { const int o2 = opaque(osw);
#pragma unroll
        for (int k = 0; k < 4; ++k) ka[k] = lds_rd128(lds + L_QKM + k * 1024 + o2); }
      SCHED();
      const float neg = __int_as_float(opaque((int)0xf149f2ca)), mk = o < (wid == 0 ? 512 : 0) ? neg : 0.f;
      f32x4 sc = {mk, mk, mk, mk};
      SCHED();
#pragma unroll
      for (int k = 0; k < 4; ++k) sc = __builtin_amdgcn_mfma_f32_16x16x32_bf16(ka[k], qb[k], sc, 0, 0, 0);
      SCHED();
      ldsp mr = lds + (o & 255), vr = lds + wid * 2048 + o;
      int so = ((o >> 8) * 64 + (o & 63) + (o & 64) * 1024) | ((o & 128) << 23);
      ldsp mw = mr + wid * 256 + (o >> 8) * 4, sw = lds + wid * 1024 + o;
      asm volatile("" : "+v"(mw), "+v"(sw), "+v"(mr), "+v"(vr), "+v"(so));
      SCHED();
      *(LAS f32x4*)(sw + L_SC) = sc;
      *(LAS float*)(mw + L_MX) = vmax3(vmax2(sc[0], sc[1]), sc[2], sc[3]);
      STAMP(5);
      LBAR();
      STAMP(6);
      f32x4 m1 = *(LAS f32x4*)(mr + L_MX + 256), m2 = *(LAS f32x4*)(mr + L_MX + 512); f32x2 m0 = *(LAS f32x2*)(mr + L_MX + 8);
      f32x4 s0 = *(LAS f32x4*)(lds + L_SC + l16), s1 = *(LAS f32x4*)(lds + L_SC + 1024 + l16), s2 = *(LAS f32x4*)(lds + L_SC + 2048 + l16);
      bf16x8 vx0 = lds_rd128(vr + L_TOK + 16384), vx1 = lds_rd128(vr + L_TOK + 16384 + 1024);
      SCHED();
      const unsigned one2 = (unsigned)opaque(0x3f803f80);
      u32x4 ones = {one2, one2, one2, one2}; asm volatile("" : "+v"(ones));
      if (__builtin_expect(!all_ne_sent(__builtin_elementwise_max(vm0[0], vm1[0])), 0))
        for (unsigned spins = 0;;) { if (spins == 0) COUNT_FAIL(3);
          vload();
          if (all_ne_sent(__builtin_elementwise_max(vm0[0], vm1[0])) || give_up(spins, rx)) break; }
      SCHED();
      STAMP(7);
      const float mx = vmax3(vmax3(m1[0], m1[1], m1[2]), vmax3(m1[3], m2[0], m2[1]), vmax3(vmax3(m2[2], m2[3], m0[0]), m0[1], m0[1])) * SC;
#pragma unroll
      for (int i = 0; i < 4; ++i) s0[i] = __builtin_fmaf(s0[i], SC, -mx);
#pragma unroll
      for (int i = 0; i < 4; ++i) s1[i] = __builtin_fmaf(s1[i], SC, -mx);
      SCHED();
#pragma unroll
      for (int i = 0; i < 4; ++i) { s0[i] = __builtin_amdgcn_exp2f(s0[i]); s2[i] = __builtin_fmaf(s2[i], SC, -mx); SCHED(); }
      s1[0] = __builtin_amdgcn_exp2f(s1[0]);
      const u32x2 pm = {pk_bf16(s0[0], s0[1]), pk_bf16(s0[2], s0[3])};
      s1[1] = __builtin_amdgcn_exp2f(s1[1]); s1[2] = __builtin_amdgcn_exp2f(s1[2]);
      SCHED();
      f32x4 ls = mfma16((u32x2){ones[0], ones[1]}, pm, z4);
      s1[3] = __builtin_amdgcn_exp2f(s1[3]); s2[0] = __builtin_amdgcn_exp2f(s2[0]);
      SCHED();
      f32x4 a0 = mfma16(vm0, pm, z4);
      s2[1] = __builtin_amdgcn_exp2f(s2[1]); s2[2] = __builtin_amdgcn_exp2f(s2[2]);
      SCHED();
      f32x4 a1 = mfma16(vm1, pm, z4);
      s2[3] = __builtin_amdgcn_exp2f(s2[3]);
      const bf16x8 px = __builtin_bit_cast(bf16x8, (u32x4){pk_bf16(s1[0], s1[1]), pk_bf16(s1[2], s1[3]), pk_bf16(s2[0], s2[1]), pk_bf16(s2[2], s2[3])});
      SCHED();
      ls = __builtin_amdgcn_mfma_f32_16x16x32_bf16(__builtin_bit_cast(bf16x8, ones), px, ls, 0, 0, 0);
      a0 = __builtin_amdgcn_mfma_f32_16x16x32_bf16(vx0, px, a0, 0, 0, 0);
      a1 = __builtin_amdgcn_mfma_f32_16x16x32_bf16(vx1, px, a1, 0, 0, 0);
      SCHED();
      const float inv = __builtin_amdgcn_rcpf(ls[0]);
      a0 *= inv; a1 *= inv;
      u32x4 ov = pack8(a0, a1); ov[0] = nosent(ov[0]); ov[2] = nosent(ov[2]);
      __builtin_amdgcn_raw_buffer_store_b128(ov, rx, so, xmap(X_O + s * 65536 + h * 1024 + wid * 256), 16);
      STAMP(8);
      __builtin_amdgcn_s_setprio(0);
      if (wid == 1 && s > 0) publish_progress(rc, C_PROG_QK + h * PW, s);
      if (wid == 0 && s + 1 < NSEG && avail < s + 2) avail = wait_progress(rc, rx, C_PROG_P + h * 2 * PW, 2, s + 2);
    } else {
      const int rt = wid & 1, eh = (wid >> 1) & 1;
      LBAR(); STAMP(4);
      STAMP(5);
      BAR();
      STAMP(6);
      __builtin_amdgcn_s_sleep(TOK_SLEEP);
      bf16x8 qb[4];
#pragma unroll
      for (int k = 0; k < 4; ++k) qb[k] = lds_rd128(lds + L_TOK + rt * 4096 + k * 1024 + o);
      bf16x8 kr[3], ka[4];
      auto kaddr = [&](int j) { return j < 4 ? lds + L_QKM + j * 1024 + osw : lds + L_TOK + 8192 + (j - 4) * 1024 + o; };
#pragma unroll
      for (int j = 0; j < 3; ++j) kr[j] = lds_rd128(kaddr(j));
      f32x4 sa[3];
#pragma unroll
      for (int j = 0; j < 12; ++j) {
        if ((j & 3) == 0) { const int ln = opaque(o) >> 4, dg = (ln & 15) - 4 * (ln >> 4), lim = j == 0 ? (ln >> 5) * 8 - 1 : j == 4 ? (rt ? 4 : dg) : (rt ? dg : -1);
          const float neg = __int_as_float(opaque((int)0xf149f2ca));
#pragma unroll
          for (int i = 0; i < 4; ++i) sa[j >> 2][i] = i <= lim ? 0.f : neg; }
        sa[j >> 2] = __builtin_amdgcn_mfma_f32_16x16x32_bf16(kr[j % 3], qb[j & 3], sa[j >> 2], 0, 0, 0);
        if (j + 3 < 12) kr[j % 3] = lds_rd128(kaddr(j + 3));
        SCHED();
      }
      const f32x4 s0 = sa[0], s1 = sa[1], s2 = sa[2];
      u32x2 vm[4];
#pragma unroll
      for (int j = 0; j < 4; ++j) vm[j] = __builtin_amdgcn_raw_buffer_load_b64(rx, vvo + j * 256, vsoff + eh * 1024, 16);
      float mx = vmax3(vmax3(vmax3(s0[0], s0[1], s0[2]), s0[3], s1[0]), vmax3(s1[1], s1[2], s1[3]), vmax3(vmax2(s2[0], s2[1]), s2[2], s2[3]));
      mx = xor32_max(xor16_max(mx)) * SC;
      float e[12];
#pragma unroll
      for (int i = 0; i < 4; ++i) { e[i] = __builtin_amdgcn_exp2f(__builtin_fmaf(s0[i], SC, -mx)); e[4 + i] = __builtin_amdgcn_exp2f(__builtin_fmaf(s1[i], SC, -mx)); e[8 + i] = __builtin_amdgcn_exp2f(__builtin_fmaf(s2[i], SC, -mx)); }
      const u32x2 pm = {pk_bf16(e[0], e[1]), pk_bf16(e[2], e[3])};
      const bf16x8 px = __builtin_bit_cast(bf16x8, (u32x4){pk_bf16(e[4], e[5]), pk_bf16(e[6], e[7]), pk_bf16(e[8], e[9]), pk_bf16(e[10], e[11])});
      float lsum = ((e[0] + e[1]) + (e[2] + e[3])) + ((e[4] + e[5]) + (e[6] + e[7])) + ((e[8] + e[9]) + (e[10] + e[11]));
      SCHED();
#pragma unroll
      for (int j = 0; j < 4; ++j) ka[j] = lds_rd128(lds + L_TOK + 16384 + (eh * 4 + j) * 1024 + o);
      const float inv = __builtin_amdgcn_rcpf(xor32_add(xor16_add(lsum)));
      SCHED();
      f32x4 ov[4];
#pragma unroll
      for (int j = 0; j < 4; ++j) ov[j] = __builtin_amdgcn_mfma_f32_16x16x32_bf16(ka[j], px, z4, 0, 0, 0);
      STAMP(7);
      for (unsigned spins = 0; !all_ne_sent(umax3(__builtin_elementwise_max(vm[0][0], vm[1][0]), vm[2][0], vm[3][0]));) { if (give_up(spins, rx)) break;
#pragma unroll
        for (int j = 0; j < 4; ++j) vm[j] = __builtin_amdgcn_raw_buffer_load_b64(rx, vvo + j * 256, vsoff + eh * 1024, 16); }
#pragma unroll
      for (int j = 0; j < 4; ++j) ov[j] = mfma16(vm[j], pm, ov[j]) * inv;
#pragma unroll
      for (int j = 0; j < 2; ++j) __builtin_amdgcn_raw_buffer_store_b128(pack8(ov[2 * j], ov[2 * j + 1]), rot, o, ((2 * s + rt) * 128 + h * 4 + eh * 2 + j) * 1024, 16);
      STAMP(8);
    }
    u32x2 hd = *(LAS u32x2*)(lds + L_HDR);
    if (__builtin_expect(!all_ne_sent(hd[0]), 0))
      for (unsigned spins = 0;;) { hd = __builtin_amdgcn_raw_buffer_load_b64(rx, 0, xmap(X_HDR + s * 256), 16); if (all_ne_sent(hd[0]) || give_up(spins, rx)) break; }
    t_mem = __builtin_amdgcn_readfirstlane(hd[1]);
    LBAR();
    if (s + 1 < NSEG) tok_dma(s + 1);
    STAMP(9);
    STAMP_FLUSH(h, s);
    TL(h, s);
  }
  WAIT_VM(0); BAR();
  if (wid == 0) publish_progress(rc, C_PROG_QK + h * PW, NSEG);
  FLUSH_FAIL();
}

DEV void role_vb(const Params& p, int wid, ldsp lds, int c) {
  const int lane = lane_id();
  const int fr = lane & 15, g = lane >> 4;
  const int rh = c & 1, cg = c >> 1;
  TL(32 + c, 64);
  bf16x16 Wv[8], Wo[16];
  { ldsp b = lds + wid * 16384; const int q = fr >> 2, r0 = 16 * (g >> 1) + 4 * (g & 1) + (q & 1) + 8 * (q >> 1);
#pragma unroll
    for (int i = 0; i < 16; ++i) { int row = i * 8 + (lane >> 3), ch = (lane & 7) ^ (((row >> 2) & 3) << 1); glds16(p.wv + ((size_t)(wid * 128 + row) * HE_ + c * 64 + ch * 8) * 2, b + i * 1024); }
    WAIT_VM(0);
    const int fv = (g & 1) + 2 * (q >> 1);
#pragma unroll
    for (int i = 0; i < 8; ++i) Wv[i] = tr_frag(b + ((i >> 2) * 64 + r0) * 128 + (((i & 3) ^ fv) * 32) + (lane & 3) * 8, 256, 4096);
    asm volatile("s_waitcnt lgkmcnt(0)" ::: "memory");
#pragma unroll
    for (int hp = 0; hp < 2; ++hp) {
#pragma unroll
      for (int i = 0; i < 16; ++i) { int row = i * 16 + (lane >> 2); glds16(p.wo + ((size_t)(wid * 512 + hp * 256 + row) * D_ + cg * 32 + (lane & 3) * 8) * 2, b + i * 1024); }
      WAIT_VM(0);
#pragma unroll
      for (int i = 0; i < 8; ++i)
        Wo[hp * 8 + i] = tr_frag(b + ((i >> 2) * 128 + 32 * (g >> 1) + 16 * (g & 1) + 8 * ((i >> 1) & 1) + 4 * (q >> 1) + (q & 1)) * 64 + (i & 1) * 32 + (lane & 3) * 8, 128, 4096);
      asm volatile("s_waitcnt lgkmcnt(0)" ::: "memory");
    }
    LBAR();
  }
  rsrc_t rx = mk_rsrc(xch_base(p.ws), XCH_BYTES);
  const int idx = (lane & 8) ? 0xEEEE : 0x4444, l16 = lane * 16;
  const f32x4 z4 = {0.f, 0.f, 0.f, 0.f};
  STAMP_DECL;
  const int idxb = (lane & 4) ? 0xEEEE : 0x4444;
  ldsp madd = lds + 16384 + wid * 512 + (g & 1) * 128 + (lane >> 5) * 64 + fr * 4;
  const int mso = ((fr >> 3) * 256 + ((fr >> 2) & 1) * 128 + (rh * 4 + g) * 16 + ((fr >> 1) & 1) * 8) | ((lane & 1) << 30);
  TL(32 + c, 65);
  unsigned t_mem = now32(), t_o = 0, nfail = 0;
  if (c == 0 && wid == 0) { unsigned a = t_mem + p.xa; __builtin_amdgcn_raw_buffer_store_b64((u32x2){nosent(a), a + p.xb}, rx, lane ? 1 << 30 : 0, xmap(X_HDR), 16); }
  for (int s = 0; s < NSEG; ++s) {
    STAMP(0);
    { u32x4 m[2]; u32x2 hd;
      if (s > 0) wait_until(t_mem);
      STAMP(10);
      gather<2, true>(p, rx, l16, xmap(s * 16384 + wid * 2048), m, xmap(X_HDR + s * 256), hd, 1, nfail);
      STAMP(1);
      bf16x8 a0 = __builtin_bit_cast(bf16x8, m[0]), a1 = __builtin_bit_cast(bf16x8, m[1]);
      f32x4 acc[4];
#pragma unroll
      for (int ct = 0; ct < 4; ++ct) acc[ct] = smf(a0, Wv[ct], z4, idx);
#pragma unroll
      for (int ct = 0; ct < 4; ++ct) acc[ct] = smf(a1, Wv[4 + ct], acc[ct], idx);
      t_o = __builtin_amdgcn_readfirstlane(hd[0]); t_mem = __builtin_amdgcn_readfirstlane(hd[1]);
#pragma unroll
      for (int pr = 0; pr < 2; ++pr) { f32x4 f;
#pragma unroll
        for (int i = 0; i < 4; ++i) f[i] = fold2(acc[2 * pr][i], acc[2 * pr + 1][i]);
        *(LAS f32x4*)(lds + (wid * 2 + pr) * 1024 + l16) = f; } }
    LBAR();
    if (wid < 2) { f32x4 t[8];
#pragma unroll
      for (int w = 0; w < 8; ++w) t[w] = *(LAS f32x4*)(lds + (w * 2 + wid) * 1024 + l16);
      f32x4 u = ((t[0] + t[1]) + (t[2] + t[3])) + ((t[4] + t[5]) + (t[6] + t[7]));
      __builtin_amdgcn_raw_buffer_store_b64((u32x2){nosent(pk_bf16(u[0], u[1])), pk_bf16(u[2], u[3])}, rx, lane * 8, xmap(X_V + s * 65536 + c * 1024 + wid * 512), 16); }
    STAMP(2);
    if (s + 1 < NSEG) {
      const int osoff = xmap(X_O + s * 65536 + rh * 32768 + wid * 4096);
      wait_until(t_o);
      STAMP(11);
      f32x4 a[4];
      for (unsigned spins = 0;;) {
        u32x4 o[4]; unsigned chk = 0;
#pragma unroll
        for (int i = 0; i < 4; ++i) { o[i] = __builtin_amdgcn_raw_buffer_load_b128(rx, l16 + i * 1024, osoff, 16); a[i] = z4; }
        SCHED();
#pragma unroll
        for (int i = 0; i < 4; ++i) {
#pragma unroll
          for (int j = 0; j < 4; ++j) a[j] = smf(__builtin_bit_cast(bf16x8, o[i]), Wo[i * 4 + j], a[j], idxb);
          chk = umax3(chk, o[i][0], o[i][2]); SCHED(); }
        if (__builtin_expect(all_ne_sent(chk), 1)) break;
        if (spins == 0) COUNT_FAIL(2);
        if (give_up(spins, rx)) break;
      }
      STAMP(3);
      { float y[4];
#pragma unroll
        for (int i = 0; i < 4; ++i) { auto r0 = __builtin_amdgcn_permlane32_swap(__float_as_uint(a[0][i]), __float_as_uint(a[1][i]), false, false), r1 = __builtin_amdgcn_permlane32_swap(__float_as_uint(a[2][i]), __float_as_uint(a[3][i]), false, false);
          y[i] = __uint_as_float(r0[0]) + __uint_as_float(r1[1]); }
#pragma unroll
        for (int i = 0; i < 4; i += 2) { auto r = __builtin_amdgcn_permlane16_swap(__float_as_uint(y[i]), __float_as_uint(y[i + 1]), false, false);
          *(LAS float*)(madd + i * 128) = __uint_as_float(r[0]) + __uint_as_float(r[1]); } }
      LBAR();
      STAMP(4);
      if (c == 0 && wid == 1) {
        unsigned n = now32() + p.dl, b = (int)(n - t_mem) > 0 ? n + p.rec : t_mem, a = b + p.xa;
        __builtin_amdgcn_raw_buffer_store_b64((u32x2){nosent(a), a + p.xb}, rx, lane ? 1 << 30 : 0, xmap(X_HDR + (s + 1) * 256), 16); }
      if (wid == 0) {
        f32x2 t[8];
#pragma unroll
        for (int w = 0; w < 8; ++w) t[w] = *(LAS f32x2*)(lds + 16384 + w * 512 + lane * 8);
        f32x2 u = ((t[0] + t[1]) + (t[2] + t[3])) + ((t[4] + t[5]) + (t[6] + t[7]));
        unsigned d = pk_bf16(u[0], u[1]), dn = __builtin_amdgcn_mov_dpp(d, 0xF5, 0xF, 0xF, true);
        __builtin_amdgcn_raw_buffer_store_b64((u32x2){nosent(d), dn}, rx, mso, xmap((s + 1) * 16384 + cg * 512), 16); }
      STAMP(5);
    }
    STAMP_FLUSH(32 + c, s);
    TL(32 + c, s);
  }
  FLUSH_FAIL();
}
DEV bf16x8 tr_frag8(ldsp a, int hi) { s16x4 u0 = lds_tr(a), u1 = lds_tr(a + hi); return __builtin_bit_cast(bf16x8, __builtin_shufflevector(u0, u1, 0, 1, 2, 3, 4, 5, 6, 7)); }
template <int NS, int NT> DEV void load_wfrags(const char* W, int ldw, ldsp stage, bf16x8 (&F)[NS][NT]) {
  const int lane = lane_id();
  constexpr int RB = NT * 32, LPR = 2 * NT, RPI = 64 / LPR, SPP = 2, DPP = SPP * NT, NP = NS / SPP, NB = 16 / DPP;
  const unsigned go = (unsigned)((lane / LPR) * ldw * 2 + (lane % LPR) * 16);
  const int ro = (lane >> 2) * RB + (lane & 3) * 8;
  auto issue = [&](int ps) {
#pragma unroll
    for (int i = 0; i < DPP; ++i) glds16(W + (size_t)(ps * SPP * 32 + i * RPI) * ldw * 2 + go, stage + ((ps % NB) * DPP + i) * 1024); };
#pragma unroll
  for (int ps = 0; ps < NB; ++ps) if (ps < NP) issue(ps);
#pragma unroll
  for (int ps = 0; ps < NP; ++ps) {
    WAIT_VM((NP - 1 - ps < NB - 1 ? NP - 1 - ps : NB - 1) * DPP);
#pragma unroll
    for (int ks = 0; ks < SPP; ++ks)
#pragma unroll
      for (int n = 0; n < NT; ++n) F[ps * SPP + ks][n] = tr_frag8(stage + (ps % NB) * DPP * 1024 + ks * 32 * RB + n * 32 + ro, 16 * RB);
    asm volatile("s_waitcnt lgkmcnt(0)" ::: "memory");
    if (ps + NB < NP) issue(ps + NB);
  }
}
#define MFMA32(a, b, c) __builtin_amdgcn_mfma_f32_16x16x32_bf16(a, b, c, 0, 0, 0)

template <int MODE> DEV void role_k1024(const Params& p, int wid, ldsp lds, int i) {
  const int lane = lane_id(), l16 = lane * 16, ng = wid & 3, kh = wid >> 2;
  const int tlr = (MODE == 0 ? 96 : 192) + i; (void)tlr;
  TL(tlr, 64);
  bf16x8 W2[16][2], W1[16][1];
  if (MODE == 0) {
    load_wfrags<16, 2>(((i & 1) ? p.wk : p.wq) + ((size_t)kh * 512 * HE_ + (i >> 1) * 128 + ng * 32) * 2, HE_, lds + wid * 16384, W2);
    load_wfrags<16, 1>(p.wv + ((size_t)kh * 512 * HE_ + i * 64 + ng * 16) * 2, HE_, lds + wid * 16384, W1);
  } else load_wfrags<16, 2>(p.w1 + ((size_t)kh * 512 * F_ + i * 128 + ng * 32) * 2, F_, lds + wid * 16384, W2);
  rsrc_t rc = mk_rsrc(p.ws + WS_CTRL, CTRL_BYTES), rx = mk_rsrc(xch_base(p.ws), XCH_BYTES);
  rsrc_t ra = mk_rsrc(p.ws + (MODE == 0 ? WS_XN : WS_HT), 4u << 20), ro = mk_rsrc(p.ws + (MODE == 0 ? WS_QKV : WS_HID), MODE == 0 ? 48u << 20 : 14u << 20), rs = mk_rsrc(p.ws + WS_STATS, 1u << 19);
  const f32x4 z4 = {0.f, 0.f, 0.f, 0.f};
  constexpr int L_EX = 131072;
  f32x4 cs[2];
  LBAR();
  if (MODE == 1) {
    const unsigned o2 = 0x3f803f80u; const bf16x8 ones = __builtin_bit_cast(bf16x8, (u32x4){o2, o2, o2, o2});
    cs[0] = z4; cs[1] = z4;
#pragma unroll
    for (int k = 0; k < 16; ++k) { cs[0] = MFMA32(W2[k][0], ones, cs[0]); cs[1] = MFMA32(W2[k][1], ones, cs[1]); }
    *(LAS f32x4*)(lds + L_EX + wid * 3072 + l16) = cs[0]; *(LAS f32x4*)(lds + L_EX + wid * 3072 + 1024 + l16) = cs[1];
    LBAR();
    cs[0] += *(LAS f32x4*)(lds + L_EX + (wid ^ 4) * 3072 + l16); cs[1] += *(LAS f32x4*)(lds + L_EX + (wid ^ 4) * 3072 + 1024 + l16);
    LBAR();
  }
  TL(tlr, 65);
  int avail = MODE == 0 ? NSEG : 0;
  auto a_dma = [&](int s) {
#pragma unroll
    for (int j = 0; j < 8; ++j) blds16_sc1(ra, l16, s * 65536 + (wid * 8 + j) * 1024, lds + (s & 1) * 65536 + (wid * 8 + j) * 1024); };
  bool fetched = false;
  for (int s = 0; s < NSEG; ++s) {
    if (!fetched) {
      if (MODE == 1) {
        if (wid == 0) *(LAS int*)(lds + L_EX + 24576 + 512) = wait_progress(rc, rx, C_PROG_O, 64, s + 1);
        LBAR();
        avail = __builtin_amdgcn_readfirstlane(*(LAS int*)(lds + L_EX + 24576 + 512)); }
      a_dma(s); }
    if (MODE == 1 && wid < 2) {
      u32x2 st[8];
#pragma unroll
      for (int j = 0; j < 8; ++j) st[j] = __builtin_amdgcn_raw_buffer_load_b64(rs, ((lane >> 4) * 8 + j) * 128 + (lane & 15) * 8, (2 * s + wid) * 4096, 16);
      float sm = 0.f;
#pragma unroll
      for (int j = 0; j < 8; ++j) sm += __uint_as_float(st[j][0]);
      sm = xor32_add(xor16_add(sm)); const float mean = sm * (1.f / 32.f); float q = 0.f;
#pragma unroll
      for (int j = 0; j < 8; ++j) { float d = __uint_as_float(st[j][0]) - mean; q += __uint_as_float(st[j][1]) + 32.f * d * d; }
      q = xor32_add(xor16_add(q));
      *(LAS f32x2*)(lds + L_EX + 24576 + (s & 1) * 256 + wid * 128 + (lane & 15) * 8) = (f32x2){mean, 1.0f / sqrtf(q * (1.f / 1024.f) + 1e-5f)};
    }
    if (!fetched) WAIT_VM(0); else if (MODE == 0 && !kh) WAIT_VM(2); else WAIT_VM(1);
    LBAR();
    if (fetched && s > 1 && wid == 7) publish_progress(rc, (MODE == 0 ? C_PROG_P : C_PROG_F1) + i * PW, s - 1);
    fetched = s > 0 && s + 1 < NSEG && avail >= s + 2;
    if (fetched) a_dma(s + 1);
    f32x4 c[2][2] = {{z4, z4}, {z4, z4}}, cv[2] = {z4, z4};
    ldsp ab = lds + (s & 1) * 65536 + kh * 16384 + l16;
    bf16x8 a[3][2];
#pragma unroll
    for (int k = 0; k < 2; ++k) { a[k][0] = lds_rd128(ab + k * 1024); a[k][1] = lds_rd128(ab + 32768 + k * 1024); }
    SCHED();
#pragma unroll
    for (int k = 0; k < 16; ++k) {
      c[0][0] = MFMA32(W2[k][0], a[k % 3][0], c[0][0]); c[0][1] = MFMA32(W2[k][1], a[k % 3][0], c[0][1]);
      if (k + 2 < 16) { a[(k + 2) % 3][0] = lds_rd128(ab + (k + 2) * 1024); a[(k + 2) % 3][1] = lds_rd128(ab + 32768 + (k + 2) * 1024); }
      c[1][0] = MFMA32(W2[k][0], a[k % 3][1], c[1][0]); c[1][1] = MFMA32(W2[k][1], a[k % 3][1], c[1][1]);
      if (MODE == 0) { cv[0] = MFMA32(a[k % 3][0], W1[k][0], cv[0]); cv[1] = MFMA32(a[k % 3][1], W1[k][0], cv[1]); }
      SCHED();
    }
    ldsp e0 = lds + L_EX + ng * 2048 + l16, e1 = lds + L_EX + 8192 + ng * 4096 + l16, ex = kh ? e1 : e0, ey = kh ? e0 : e1;
    *(LAS f32x4*)ex = kh ? c[0][0] : c[1][0]; *(LAS f32x4*)(ex + 1024) = kh ? c[0][1] : c[1][1];
    if (MODE == 0 && kh) { *(LAS f32x4*)(ex + 2048) = cv[0]; *(LAS f32x4*)(ex + 3072) = cv[1]; }
    LBAR();
    f32x4 r0 = (kh ? c[1][0] : c[0][0]) + *(LAS f32x4*)ey, r1 = (kh ? c[1][1] : c[0][1]) + *(LAS f32x4*)(ey + 1024);
    if (MODE == 0) {
      const int base = (s * 32 + (i >> 1)) * 24576;
      __builtin_amdgcn_raw_buffer_store_b128(pack8(r0, r1), ro, l16, base + (i & 1) * 8192 + (kh * 4 + ng) * 1024, 16);
      if (!kh) __builtin_amdgcn_raw_buffer_store_b128(pack8(cv[0] + *(LAS f32x4*)(ey + 2048), cv[1] + *(LAS f32x4*)(ey + 3072)), ro, l16, base + 16384 + ((i & 1) * 4 + ng) * 1024, 16);
    } else {
      const f32x2 st = *(LAS f32x2*)(lds + L_EX + 24576 + (s & 1) * 256 + kh * 128 + (lane & 15) * 8);
#pragma unroll
      for (int j = 0; j < 4; ++j) {
        float v = (r0[j] - st[0] * cs[0][j]) * st[1]; r0[j] = v * __builtin_amdgcn_rcpf(1.0f + __builtin_amdgcn_exp2f(-1.44269504f * v));
        v = (r1[j] - st[0] * cs[1][j]) * st[1]; r1[j] = v * __builtin_amdgcn_rcpf(1.0f + __builtin_amdgcn_exp2f(-1.44269504f * v)); }
      __builtin_amdgcn_raw_buffer_store_b128(pack8(r0, r1), ro, l16, ((2 * s + kh) * 112 + i * 4 + ng) * 1024, 16);
    }
    TL(tlr, s);
    if (!fetched) {
      WAIT_VM(0); LBAR();
      if (wid == 7) publish_progress(rc, (MODE == 0 ? C_PROG_P : C_PROG_F1) + i * PW, s + 1);
    }
  }
}

template <int MODE> DEV void role_longk(const Params& p, int wid, ldsp lds, int cg) {
  constexpr int NS = MODE == 0 ? 16 : 14, KG = MODE == 0 ? 128 : 112;
  const int lane = lane_id(), l16 = lane * 16;
  const int tlr = (MODE == 0 ? 160 : 220) + cg; (void)tlr;
  TL(tlr, 64);
  bf16x8 W[NS][2];
  load_wfrags<NS, 2>((MODE == 0 ? p.wo : p.w2) + ((size_t)wid * NS * 32 * D_ + cg * 32) * 2, D_, lds + wid * 16384, W);
  rsrc_t rc = mk_rsrc(p.ws + WS_CTRL, CTRL_BYTES), rx = mk_rsrc(xch_base(p.ws), XCH_BYTES);
  rsrc_t ra = mk_rsrc(p.ws + (MODE == 0 ? WS_OT : WS_HID), MODE == 0 ? 16u << 20 : 14u << 20);
  rsrc_t rr = mk_rsrc(p.ws + (MODE == 0 ? WS_XT : WS_HT), 4u << 20), rh = mk_rsrc(MODE == 0 ? p.ws + WS_HT : p.out, 4u << 20), rs = mk_rsrc(p.ws + WS_STATS, 1u << 19);
  const f32x4 z4 = {0.f, 0.f, 0.f, 0.f};
  LBAR();
  TL(tlr, 65);
  int avail = 0;
  const int p0 = wid * NS / 4, pn = (wid * NS + NS - 1) / 4 - p0 + 1, poff = (MODE == 0 ? C_PROG_QK : C_PROG_F1) + p0 * PW;
  constexpr int PD = NS / 2;
  u32x4 a[PD][2];
  auto aload = [&](int s, int k) { const int ao = (2 * s * KG + wid * NS) * 1024 + (k >> 2) * 4096;
    a[k % PD][0] = __builtin_amdgcn_raw_buffer_load_b128(ra, l16 + (k & 3) * 1024, ao, 16); a[k % PD][1] = __builtin_amdgcn_raw_buffer_load_b128(ra, l16 + (k & 3) * 1024, ao + KG * 1024, 16); };
  bool pend = false;
  avail = wait_progress(rc, rx, poff, pn, 1);
#pragma unroll
  for (int k = 0; k < PD; ++k) aload(0, k);
  for (int s = 0; s < NSEG; ++s) {
    u32x4 res = __builtin_amdgcn_raw_buffer_load_b128(rr, l16, ((2 * s + (wid & 1)) * 32 + cg) * 1024, 16);
    int fresh = __builtin_amdgcn_raw_buffer_load_b32(rc, (lane < pn ? lane : 0) * PW, poff, 16);
    SCHED();
    f32x4 c[2][2] = {{z4, z4}, {z4, z4}};
    auto mm = [&](int k) { const bf16x8 a0 = __builtin_bit_cast(bf16x8, a[k % PD][0]), a1 = __builtin_bit_cast(bf16x8, a[k % PD][1]);
      c[0][0] = MFMA32(W[k][0], a0, c[0][0]); c[0][1] = MFMA32(W[k][1], a0, c[0][1]); c[1][0] = MFMA32(W[k][0], a1, c[1][0]); c[1][1] = MFMA32(W[k][1], a1, c[1][1]); };
#pragma unroll
    for (int k = 0; k < PD; ++k) { mm(k); aload(s, k + PD); SCHED(); }
    if (avail < s + 2 && __builtin_amdgcn_ballot_w64(fresh < s + 2) == 0ull) avail = s + 2;
    if (MODE == 0 && pend) { WAIT_VM(2 * PD); publish_progress(rc, C_PROG_O + (wid * 32 + cg) * PW, s); pend = false; }
    const bool next = s + 1 < NSEG && avail >= s + 2;
    if (next) {
#pragma unroll
      for (int k = PD; k < NS; ++k) { mm(k); aload(s + 1, k - PD); SCHED(); }
    } else {
#pragma unroll
      for (int k = PD; k < NS; ++k) { mm(k); SCHED(); }
    }
    ldsp red = lds + (s & 1) * 32768 + l16;
#pragma unroll
    for (int j = 0; j < 4; ++j) *(LAS f32x4*)(red + (wid * 4 + j) * 1024) = c[j >> 1][j & 1];
    LBAR();
    if (wid < 2) {
      f32x4 r0 = z4, r1 = z4;
#pragma unroll
      for (int w = 0; w < 8; ++w) { r0 += *(LAS f32x4*)(red + (w * 4 + wid * 2) * 1024); r1 += *(LAS f32x4*)(red + (w * 4 + wid * 2 + 1) * 1024); }
      u32x4 o = pack8(r0, r1), hv;
#pragma unroll
      for (int j = 0; j < 4; ++j) hv[j] = pk_bf16(bf_lo(o[j]) + bf_lo(res[j]), bf_hi(o[j]) + bf_hi(res[j]));
      if (MODE == 0) {
        __builtin_amdgcn_raw_buffer_store_b128(hv, rh, l16, ((2 * s + wid) * 32 + cg) * 1024, 16);
        float sm = 0.f;
#pragma unroll
        for (int j = 0; j < 4; ++j) sm += bf_lo(hv[j]) + bf_hi(hv[j]);
        sm = xor32_add(xor16_add(sm)); const float mean = sm * (1.f / 32.f); float q = 0.f;
#pragma unroll
        for (int j = 0; j < 4; ++j) { float d0 = bf_lo(hv[j]) - mean, d1 = bf_hi(hv[j]) - mean; q += d0 * d0 + d1 * d1; }
        q = xor32_add(xor16_add(q));
        __builtin_amdgcn_raw_buffer_store_b64((u32x2){__float_as_uint(mean), __float_as_uint(q)}, rs, lane < 16 ? lane * 8 : 1 << 30, ((2 * s + wid) * 32 + cg) * 128, 16);
        if (next) pend = true; else { WAIT_VM(0); publish_progress(rc, C_PROG_O + (wid * 32 + cg) * PW, s + 1); }
      } else {
        const int ro = (lane & 15) * 2048 + (lane >> 4) * 8, so = (32 * s + 16 * wid) * 2048 + cg * 64;
        __builtin_amdgcn_raw_buffer_store_b64((u32x2){hv[0], hv[1]}, rh, ro, so, 0);
        __builtin_amdgcn_raw_buffer_store_b64((u32x2){hv[2], hv[3]}, rh, ro + 32, so, 0);
      }
    }
    TL(tlr, s);
    if (!next && s + 1 < NSEG) {
      avail = wait_progress(rc, rx, poff, pn, s + 2);
#pragma unroll
      for (int k = 0; k < PD; ++k) aload(s + 1, k);
    }
  }
}
#define LATE_DELAY 600
constexpr int LDS_BYTES = L_QK_END;
__global__ void __launch_bounds__(512, 2) k_main(Params p) {
  extern __shared__ __attribute__((aligned(16))) char smem[];
  ldsp lds = (ldsp)smem;
  const int wid = __builtin_amdgcn_readfirstlane(threadIdx.x >> 6);
  if (threadIdx.x == 0) {
    GAS unsigned* tk = (GAS unsigned*)(p.ws + WS_CTRL + C_TICKET);
    const unsigned x = xcc_id(); unsigned t = __hip_atomic_fetch_add(tk + x * 64, 1u, RLX_AGENT), id = x * 32 + t;
    if (t >= 32) {
      const unsigned j = __hip_atomic_fetch_add(tk + 32, 1u, RLX_AGENT); unsigned cnt[8];
      for (unsigned spins = 0; spins < (1u << 22); ++spins) { unsigned sum = 0;
#pragma unroll
        for (int q = 0; q < 8; ++q) { cnt[q] = __hip_atomic_load(tk + q * 64, RLX_AGENT); sum += cnt[q]; }
        if (sum >= 256u) break; }
      unsigned left = j;
#pragma unroll
      for (int q = 7; q >= 0; --q) { const unsigned fr = cnt[q] < 32u ? 32u - cnt[q] : 0u; if (left < fr) { id = q * 32 + cnt[q] + left; left = ~0u; } else if (left != ~0u) left -= fr; }
    }
    *(LAS unsigned*)lds = id ^ (*(const unsigned*)(p.ws + WS_CTRL + C_IOD) << 7);
  }
  LBAR();
  const int id = __builtin_amdgcn_readfirstlane(*(LAS int*)lds), xg = id >> 5, slot = id & 31;
  LBAR();
  if (xg < 3) { const int role = ((slot >> 2) * 3 + xg) * 4 + (slot & 3); if (role < 32) role_qk(p, wid, lds, role); else role_vb(p, wid, lds, role - 32); }
  else { const int j = ((slot >> 2) * 5 + (xg - 3)) * 4 + (slot & 3);
    if (j < 64) role_k1024<0>(p, wid, lds, j);
    else {
      { const unsigned t = now32() + LATE_DELAY; while ((int)(t - now32()) > 0) __builtin_amdgcn_s_sleep(16); }
      if (j < 96) role_longk<0>(p, wid, lds, j - 64);
      else if (j < 124) role_k1024<1>(p, wid, lds, j - 96);
      else if (j < 156) role_longk<1>(p, wid, lds, j - 124); } }
}
unsigned g_xa = 175, g_xb = 102, g_dl = 15;
extern "C" void kernel_launch(void* const* d_in, const int* in_sizes, int n_in, void* d_out, int out_size, void* d_ws, size_t ws_size, hipStream_t stream) {
  Params p{(const char*)d_in[0], (const char*)d_in[1], (const char*)d_in[2], (const char*)d_in[3], (const char*)d_in[4], (const char*)d_in[5], (const char*)d_in[8], (const char*)d_in[9], (char*)d_out, (char*)d_ws, g_xa, g_xb, g_dl, 120u};
  (void)hipFuncSetAttribute((const void*)k_main, hipFuncAttributeMaxDynamicSharedMemorySize, LDS_BYTES);
  k_prologue<<<256, 512, 0, stream>>>(p);
  k_main<<<256, 512, LDS_BYTES, stream>>>(p);
}
```
